```python
import torch

def _softmax(x, axis=-1):
    return torch.softmax(x, dim=axis)
def _gelu(x):
    return torch.nn.functional.gelu(x, approximate="tanh")
def _silu(x):
    return torch.nn.functional.silu(x)
def _sigmoid(x):
    return torch.sigmoid(x)
def _layernorm(x, axis=-1, eps=1e-5):
    m = x.mean(dim=axis, keepdim=True)
    v = x.var(dim=axis, keepdim=True, unbiased=False)
    return (x - m) / torch.sqrt(v + eps)
def _scatter_add(tgt, idx, src, axis=0):
    return tgt.clone().index_add_(axis, idx, src)
def _segment_sum(src, seg, n):
    out = torch.zeros((n,) + src.shape[1:], dtype=src.dtype)
    return out.index_add_(0, seg, src)
def _pad(x, pw):
    flat = tuple(v for pair in reversed(pw) for v in pair)
    return torch.nn.functional.pad(x, flat)

kernel_name = 'sequential_preLN__sliding_local_dilated__mixer_token_mlp__prompt'

def setup_inputs():

    _w = (lambda a: (lambda t: t.to(torch.bfloat16) if t.is_floating_point() else t)(torch.as_tensor(a)))
    def _A_setup():
        B, S, D = 1, 2048, 640
        return {
            "x": _w((lambda *s: torch.randn(*s, dtype=torch.bfloat16) * 0.05)(B, S, D)),
            "wqkv": _w((lambda *s: torch.randn(*s, dtype=torch.bfloat16) * 0.05)(D, 3 * D)),
            "wo": _w((lambda *s: torch.randn(*s, dtype=torch.bfloat16) * 0.05)(D, D)),
        }
    _A_in = _A_setup()

    def _B_setup():
        B, S, D, F = 1, 2048, 640, 1536
        return {
            "x": _w((lambda *s: torch.randn(*s, dtype=torch.bfloat16) * 0.05)(B, S, D)),
            "w1": _w((lambda *s: torch.randn(*s, dtype=torch.bfloat16) * 0.8)(S, F) * (S ** -0.5)),
            "b1": _w((lambda *s: torch.randn(*s, dtype=torch.bfloat16) * 0.8)(F) * 0.02),
            "w2": _w((lambda *s: torch.randn(*s, dtype=torch.bfloat16) * 0.8)(F, S) * (F ** -0.5)),
            "b2": _w((lambda *s: torch.randn(*s, dtype=torch.bfloat16) * 0.8)(S) * 0.02),
        }
    _B_in = _B_setup()

    return {
        "A_x": _A_in["x"],
        "A_wqkv": _A_in["wqkv"],
        "A_wo": _A_in["wo"],
        "B_x": _B_in["x"],
        "B_w1": _B_in["w1"],
        "B_b1": _B_in["b1"],
        "B_w2": _B_in["w2"],
        "B_b2": _B_in["b2"],
    }

def reference(A_x, A_wqkv, A_wo, B_x, B_w1, B_b1, B_w2, B_b2):
    def _A_ref(x, wqkv, wo):
        B, S, D = x.shape
        H = 32; Dh = D // H
        W, dil = 8, 2
        qkv = (lambda x, *d: x.permute(*d))((x @ wqkv).reshape(B, S, 3, H, Dh), 2, 0, 3, 1, 4)
        q, k, v = qkv[0], qkv[1], qkv[2]
        scale = Dh ** -0.5
        pos = torch.arange(S)
        off = torch.arange(W) * dil
        idx = pos[:, None] - off[None, :]
        mask = idx >= 0
        idx_c = torch.where(mask, idx, 0).reshape(-1)
        kg = k[:, :, idx_c, :].reshape(B, H, S, W, Dh)
        vg = v[:, :, idx_c, :].reshape(B, H, S, W, Dh)
        logits = torch.einsum("bhsd,bhswd->bhsw", q, kg) * scale
        logits = torch.where(mask[None, None, :, :], logits, -1e9)
        attn = _softmax(logits, axis=-1)
        o = torch.einsum("bhsw,bhswd->bhsd", attn, vg)
        return (lambda x, *d: x.permute(*d))(o, 0, 2, 1, 3).reshape(B, S, D) @ wo

    def _B_ref(x, w1, b1, w2, b2):
        B, S, D = x.shape
        h = _layernorm(x, axis=-1)
        u = torch.einsum("bsd,sf->bdf", h, w1) + b1
        u = _gelu(u)
        y = torch.einsum("bdf,fs->bds", u, w2) + b2
        return (lambda x, *d: x.permute(*d))(y, 0, 2, 1)

    x = A_x
    h = x + _A_ref(_layernorm(x, axis=-1), A_wqkv, A_wo)
    h2 = h + _B_ref(_layernorm(h, axis=-1), B_w1, B_b1, B_w2, B_b2)
    return h2
```

```cpp
#include <hip/hip_runtime.h>
#include <hip/hip_bf16.h>
typedef unsigned short u16;
typedef short bf16x8 __attribute__((ext_vector_type(8)));
typedef float f32x4 __attribute__((ext_vector_type(4)));
constexpr int S=2048, D=640, F=1536, H=32, DH=20;
__device__ __forceinline__ float bf2f(u16 b){ return __builtin_bit_cast(float,(unsigned)b<<16); }
__device__ __forceinline__ u16 f2bf(float f){ __hip_bfloat16 h=__float2bfloat16(f); return __builtin_bit_cast(u16,h); }

template<bool INF32,int TIMES>
__global__ void ln_kernel(const void* in, u16* out){
  int row=blockIdx.x; int t=threadIdx.x;
  float v[10];
  for(int i=0;i<10;i++){ int c=t+64*i; v[i]= INF32? ((const float*)in)[row*D+c] : bf2f(((const u16*)in)[row*D+c]); }
  for(int rep=0;rep<TIMES;rep++){
    float s=0; for(int i=0;i<10;i++) s+=v[i];
    for(int o=32;o;o>>=1) s+=__shfl_xor(s,o);
    float m=s/D; float q=0; for(int i=0;i<10;i++){ float d=v[i]-m; q+=d*d; }
    for(int o=32;o;o>>=1) q+=__shfl_xor(q,o);
    float r=1.0f/sqrtf(q/D+1e-5f);
    for(int i=0;i<10;i++) v[i]=(v[i]-m)*r;
  }
  for(int i=0;i<10;i++) out[row*D+t+64*i]=f2bf(v[i]);
}
template<int EPI>
__global__ void gemm_kernel(const u16* A,int lam,int lak,const u16* B,int ldb,int K,
                            void* C,int ldc,const u16* bias,const void* res){
  int lane=threadIdx.x; int n0=blockIdx.x*16, m0=blockIdx.y*16;
  f32x4 acc={0,0,0,0};
  int r=lane&15,q=lane>>4;
  for(int k0=0;k0<K;k0+=32){
    bf16x8 a,b;
    for(int j=0;j<8;j++){ int k=k0+q*8+j; a[j]=(short)A[(size_t)(m0+r)*lam+(size_t)k*lak]; b[j]=(short)B[(size_t)k*ldb+n0+r]; }
    acc=__builtin_amdgcn_mfma_f32_16x16x32_bf16(a,b,acc,0,0,0);
  }
  for(int j=0;j<4;j++){
    int m=m0+q*4+j, n=n0+r; float v=acc[j];
    if(EPI==0){ ((float*)C)[(size_t)m*ldc+n]=v; }
    else if(EPI==1){ ((float*)C)[(size_t)m*ldc+n]=bf2f(f2bf(v+bf2f(((const u16*)res)[(size_t)m*ldc+n]))); }
    else if(EPI==2){ v+=bf2f(bias[n]); float g=0.5f*v*(1.0f+tanhf(0.7978845608028654f*(v+0.044715f*v*v*v))); ((u16*)C)[(size_t)m*ldc+n]=f2bf(g); }
    else { v+=bf2f(bias[n]); ((u16*)C)[(size_t)n*ldc+m]=f2bf(v+((const float*)res)[(size_t)n*ldc+m]); }
  }
}
__global__ void attn_kernel(const float* qkv,u16* o){
  int idx=blockIdx.x*blockDim.x+threadIdx.x; int s=idx/H,h=idx%H;
  float lg[8]; float mx=-1e30f;
  for(int w=0;w<8;w++){ int j=s-2*w; float a=-1e9f; if(j>=0){ a=0; for(int d=0;d<DH;d++) a+=qkv[(size_t)s*1920+h*DH+d]*qkv[(size_t)j*1920+640+h*DH+d]; a*=0.22360679774997896f; } lg[w]=a; mx=fmaxf(mx,a);}
  float sum=0; for(int w=0;w<8;w++){ lg[w]=__expf(lg[w]-mx); sum+=lg[w]; }
  for(int d=0;d<DH;d++){ float a=0; for(int w=0;w<8;w++){ int j=s-2*w; if(j<0) j=0; a+=lg[w]*qkv[(size_t)j*1920+1280+h*DH+d]; } o[(size_t)s*D+h*DH+d]=f2bf(a/sum); }
}
extern "C" void kernel_launch(void* const* d_in, const int* in_sizes, int n_in, void* d_out, int out_size, void* d_ws, size_t ws_size, hipStream_t stream){
  const u16* x=(const u16*)d_in[0]; const u16* wqkv=(const u16*)d_in[1]; const u16* wo=(const u16*)d_in[2];
  const u16* w1=(const u16*)d_in[4]; const u16* b1=(const u16*)d_in[5]; const u16* w2=(const u16*)d_in[6]; const u16* b2=(const u16*)d_in[7];
  char* ws=(char*)d_ws;
  u16* xn=(u16*)ws; float* qkv=(float*)(ws+(4<<20)); u16* o=(u16*)(ws+(24<<20)); float* h=(float*)(ws+(28<<20)); u16* hn=(u16*)(ws+(36<<20)); u16* u=(u16*)(ws+(40<<20));
  ln_kernel<false,1><<<S,64,0,stream>>>(x,xn);
  gemm_kernel<0><<<dim3(1920/16,S/16),64,0,stream>>>(xn,D,1,wqkv,1920,D,qkv,1920,nullptr,nullptr);
  attn_kernel<<<S*H/256,256,0,stream>>>(qkv,o);
  gemm_kernel<1><<<dim3(D/16,S/16),64,0,stream>>>(o,D,1,wo,D,D,h,D,nullptr,x);
  ln_kernel<true,2><<<S,64,0,stream>>>(h,hn);
  gemm_kernel<2><<<dim3(F/16,D/16),64,0,stream>>>(hn,1,D,w1,F,S,u,F,b1,nullptr);
  gemm_kernel<3><<<dim3(S/16,D/16),64,0,stream>>>(u,F,1,w2,S,F,d_out,D,b2,h);
}
```

```cpp
#include <hip/hip_runtime.h>
#include <hip/hip_bf16.h>

typedef unsigned short u16;
typedef unsigned v4u __attribute__((ext_vector_type(4)));
typedef unsigned v2u __attribute__((ext_vector_type(2)));
typedef float f32x4 __attribute__((ext_vector_type(4)));
typedef short bf16x8 __attribute__((ext_vector_type(8)));
typedef short s16x4 __attribute__((ext_vector_type(4)));
typedef __bf16 bf2 __attribute__((ext_vector_type(2)));
#define LAS __attribute__((address_space(3)))
#define GAS __attribute__((address_space(1)))
typedef LAS char* lptr;
typedef __amdgpu_buffer_rsrc_t rsrc_t;

#define BAR() asm volatile("s_waitcnt lgkmcnt(0)\n\ts_barrier" ::: "memory")
#define DRAIN() asm volatile("s_waitcnt vmcnt(0)" ::: "memory")
#define SB() __builtin_amdgcn_sched_barrier(0)
#define DEV __device__ __forceinline__

constexpr int SC1 = 16;
constexpr int OOB = 0x7ffffff0;
constexpr int WS_EPOCH = 0;
constexpr int WS_F1 = 4096, WS_F2 = WS_F1 + 32768, WS_F3 = WS_F2 + 32768;
constexpr int WS_ST = 131072;
constexpr int WS_O = 1 << 20;
constexpr int WS_HN = 4 << 20;
constexpr int WS_U = 8 << 20;
constexpr int WS_BYTES = 12 << 20;
constexpr int LDS_BYTES = 163840;
constexpr int L_WRING = 102400;
constexpr int L_QKV = 102400, QRS = 488;
constexpr int L_SLAB = 141440;
constexpr int L_RED2 = 102400;
constexpr int L_HN = 0;
constexpr int EARLY_W1 = 8;
constexpr int SPIN_CAP = 1 << 16;

#define STAMP(k)

DEV v4u ld128(rsrc_t r, int v, int s) { return __builtin_amdgcn_raw_buffer_load_b128(r, v, s, 0); }
DEV v4u ld128c(rsrc_t r, int v, int s) { return __builtin_amdgcn_raw_buffer_load_b128(r, v, s, SC1); }
DEV void st128c(v4u d, rsrc_t r, int v, int s) { __builtin_amdgcn_raw_buffer_store_b128(d, r, v, s, SC1); }
DEV f32x4 mfma(v4u a, v4u b, f32x4 c) {
  return __builtin_amdgcn_mfma_f32_16x16x32_bf16(__builtin_bit_cast(bf16x8, a), __builtin_bit_cast(bf16x8, b), c, 0, 0, 0);
}
DEV unsigned pk(float lo, float hi) { bf2 v; v[0] = (__bf16)lo; v[1] = (__bf16)hi; return __builtin_bit_cast(unsigned, v); }
DEV float asf(unsigned u) { return __builtin_bit_cast(float, u); }
DEV unsigned asu(float f) { return __builtin_bit_cast(unsigned, f); }
DEV bf2 asbf2(unsigned u) { return __builtin_bit_cast(bf2, u); }
DEV float blo(unsigned u) { return __builtin_bit_cast(float, u << 16); }
DEV float bhi(unsigned u) { return __builtin_bit_cast(float, u & 0xffff0000u); }
DEV v2u trrd(lptr p) { return __builtin_bit_cast(v2u, __builtin_amdgcn_ds_read_tr16_b64_v4i16((LAS s16x4*)p)); }
template <int CTRL> DEV float dppf(float x) {
  return __builtin_bit_cast(float, __builtin_amdgcn_update_dpp(0, __builtin_bit_cast(int, x), CTRL, 0xF, 0xF, true));
}
DEV float sum16(float x) { x += dppf<0x128>(x); x += dppf<0x124>(x); x += dppf<0x122>(x); x += dppf<0x121>(x); return x; }
DEV float sumg(float x) {
  auto r = __builtin_amdgcn_permlane16_swap(asu(x), asu(x), false, false);
  x = asf(r[0]) + asf(r[1]);
  auto q = __builtin_amdgcn_permlane32_swap(asu(x), asu(x), false, false);
  return asf(q[0]) + asf(q[1]);
}
DEV float maxg(float x) {
  auto r = __builtin_amdgcn_permlane16_swap(asu(x), asu(x), false, false);
  x = fmaxf(asf(r[0]), asf(r[1]));
  auto q = __builtin_amdgcn_permlane32_swap(asu(x), asu(x), false, false);
  return fmaxf(asf(q[0]), asf(q[1]));
}
DEV void dma(rsrc_t r, int voff, int soff, int ldsaddr) {
  asm volatile("s_mov_b32 m0, %0\n\ts_nop 0\n\tbuffer_load_dwordx4 %1, %2, %3 offen lds" ::"s"(ldsaddr), "v"(voff), "s"(r), "s"(soff) : "memory");
}
template <int N> DEV void waitv() { asm volatile("s_waitcnt vmcnt(%0)" ::"i"(N) : "memory"); }
DEV int swap23(int r) { return (r & ~12) | ((r & 4) << 1) | ((r & 8) >> 1); }

DEV void xpose(const v4u (&raw)[8], v4u (&fx)[4], v4u (&fy)[4]) {
  unsigned X[8][2], Y[8][2];
#pragma unroll
  for (int e = 0; e < 8; ++e)
#pragma unroll
    for (int t = 0; t < 2; ++t) {
      X[e][t] = __builtin_amdgcn_update_dpp(raw[e][t], raw[e][2 + t], 0x128, 0xF, 0xC, false);
      Y[e][t] = __builtin_amdgcn_update_dpp(raw[e][2 + t], raw[e][t], 0x128, 0xF, 0x3, false);
    }
#pragma unroll
  for (int j = 0; j < 4; ++j)
#pragma unroll
    for (int m = 0; m < 4; ++m) {
      const unsigned sel = (j & 1) ? 0x07060302u : 0x05040100u;
      fx[j][m] = __builtin_amdgcn_perm(X[2 * m + 1][j >> 1], X[2 * m][j >> 1], sel);
      fy[j][m] = __builtin_amdgcn_perm(Y[2 * m + 1][j >> 1], Y[2 * m][j >> 1], sel);
    }
  asm volatile("" : "+v"(fx[0]), "+v"(fx[1]), "+v"(fx[2]), "+v"(fx[3]), "+v"(fy[0]), "+v"(fy[1]), "+v"(fy[2]), "+v"(fy[3]));
}

DEV void wait_flags(const char* ws, int off, unsigned tag) {
  const GAS unsigned* p = (const GAS unsigned*)(ws + off);
#pragma nounroll
  for (int sp = 0; sp < SPIN_CAP; ++sp) {
    const unsigned v = __hip_atomic_load(p, __ATOMIC_RELAXED, __HIP_MEMORY_SCOPE_AGENT);
    if (__all(v == tag)) break;
    __builtin_amdgcn_s_sleep(1);
  }
  __builtin_amdgcn_fence(__ATOMIC_ACQUIRE, "wavefront");
}
DEV void set_flag(char* ws, int off, unsigned tag) {
  __hip_atomic_store((GAS unsigned*)(ws + off), tag, __ATOMIC_RELAXED, __HIP_MEMORY_SCOPE_AGENT);
}

__global__ void __launch_bounds__(512, 2)
fused_kernel(const u16* __restrict__ x, const u16* __restrict__ wqkv, const u16* __restrict__ wo,
             const u16* __restrict__ w1, const u16* __restrict__ b1, const u16* __restrict__ w2,
             const u16* __restrict__ b2, u16* __restrict__ out, char* ws
) {
  extern __shared__ __attribute__((aligned(16))) char smem[];
  const lptr L = (lptr)smem;
  const int tid = threadIdx.x, lane = tid & 63, w = __builtin_amdgcn_readfirstlane(tid >> 6);
  const int l16 = lane & 15, g = lane >> 4;
  const int b = blockIdx.x, xcd = b & 7, jb = b >> 3, xi = xcd >> 1, xc = xcd & 1;
  const int ti = xi * 8 + (jb >> 2), c = xc * 4 + (jb & 3);
  const int dt = xc * 5 + jb % 5, ft = xi * 6 + jb / 5;
  const bool p3on = jb < 30;
  STAMP(0);
  const rsrc_t rX = __builtin_amdgcn_make_buffer_rsrc((void*)x, 0, 2048 * 640 * 2, 0x00020000);
  const rsrc_t rWq = __builtin_amdgcn_make_buffer_rsrc((void*)wqkv, 0, 640 * 1920 * 2, 0x00020000);
  const rsrc_t rWo = __builtin_amdgcn_make_buffer_rsrc((void*)wo, 0, 640 * 640 * 2, 0x00020000);
  const rsrc_t rW1 = __builtin_amdgcn_make_buffer_rsrc((void*)w1, 0, 2048 * 1536 * 2, 0x00020000);
  const rsrc_t rW2 = __builtin_amdgcn_make_buffer_rsrc((void*)w2, 0, 1536 * 2048 * 2, 0x00020000);
  const rsrc_t rWS = __builtin_amdgcn_make_buffer_rsrc((void*)ws, 0, WS_BYTES, 0x00020000);

  unsigned code_v;
  {
    unsigned long long pc;
    int len;
    asm volatile("s_getpc_b64 %0\n.Lk_begin:\n\ts_mov_b32 %1, .Lk_end-.Lk_begin" : "=s"(pc), "=s"(len));
    const rsrc_t rC = __builtin_amdgcn_make_buffer_rsrc((void*)pc, 0, len, 0x00020000);
    code_v = __builtin_amdgcn_raw_buffer_load_b32(rC, lane * 4, (jb * 8 + w) * 256, 0);
  }
  const unsigned epoch_v = __builtin_amdgcn_raw_buffer_load_b32(rWS, WS_EPOCH, 0, SC1);
  const int rot = ((ti & 7) * 5) >> 1;
  v4u xr[3][5];
  int xT[3];
  const int xidx = 4 * ((w + (c & 3)) & 3) + g;
#pragma unroll
  for (int q = 0; q < 3; ++q) {
    int rq = w + 8 * q + 5 * (c & 3);
    rq = rq >= 20 ? rq - 20 : rq;
    const int T = rq >> 2;
    xT[q] = T;
    int s;
    if (T == 0) s = xidx < 8 ? 64 * ti - 16 + 2 * xidx : 64 * ti - 31 + 2 * xidx;
    else s = 64 * ti + ((T - 1) & 1) * 32 + 2 * xidx + ((T - 1) >> 1);
    s = s < 0 ? 0 : s;
    const int vo = (q == 2 && w >= 4) ? OOB : s * 1280 + l16 * 16;
#pragma unroll
    for (int a = 0; a < 5; ++a) xr[q][a] = ld128(rX, vo + a * 256, 0);
    SB();
  }
  int wv[2];
#pragma unroll
  for (int k = 0; k < 2; ++k) {
    const int q = (w + 8 * k) * 64 + lane, row = q / 30, cc = q - row * 30, seg = cc / 10, ch = cc - seg * 10;
    wv[k] = (k == 1 && w == 7) ? OOB : swap23(row) * 3840 + seg * 1280 + c * 160 + ch * 16;
  }
  const int wl0 = L_WRING + w * 1024, wl1 = w == 7 ? LDS_BYTES : L_WRING + (w + 8) * 1024;
#define DMA_W(kap, k) do { int ks_ = (kap) + rot; ks_ = ks_ >= 20 ? ks_ - 20 : ks_; \
    dma(rWq, wv[k], ks_ * (32 * 3840), ((k) ? wl1 : wl0) + ((kap) & 3) * 15360); } while (0)
  const int c8 = lane & 7, hi = (lane >> 3) & 1;
  const int w1v = p3on ? (32 * hi + 8 * g) * 3072 + c8 * 16 : OOB;
  const int rot3 = jb % 5;
  v4u w1r[4][8];
#define LD_W1(sg, e) w1r[sg][e] = ld128(rW1, w1v, (256 * w + 64 * (((sg) + rot3) & 3) + (e)) * 3072 + ft * 128)
  DMA_W(0, 0); DMA_W(0, 1);
  STAMP(1);
  int xo[5];
  {
    const int gg = l16 & 3;
#pragma unroll
    for (int a = 0; a < 5; ++a) {
      int kap = 4 * a + (l16 >> 2) - rot;
      kap = kap < 0 ? kap + 20 : kap;
      xo[a] = kap * 5120 + (gg * 16 + (xidx ^ (gg | ((kap & 1) * 12)))) * 16;
    }
  }
#pragma unroll
  for (int q = 0; q < 3; ++q) {
    if (q < 2 || w < 4) {
      float s1 = 0.f, s2 = 0.f;
#pragma unroll
      for (int a = 0; a < 5; ++a)
#pragma unroll
        for (int t = 0; t < 4; ++t) {
          const bf2 v = asbf2(xr[q][a][t]);
          s1 = __builtin_amdgcn_fdot2_f32_bf16(v, asbf2(0x3f803f80u), s1, false);
          s2 = __builtin_amdgcn_fdot2_f32_bf16(v, v, s2, false);
        }
      s1 = sum16(s1); s2 = sum16(s2);
      const float mu = s1 * (1.f / 640.f);
      const float var = fmaxf(s2 * (1.f / 640.f) - mu * mu, 0.f);
      const float rs = __builtin_amdgcn_rsqf(var + 1e-5f), nb = -mu * rs;
#pragma unroll
      for (int a = 0; a < 5; ++a) {
        v4u o;
#pragma unroll
        for (int t = 0; t < 4; ++t) o[t] = pk(fmaf(blo(xr[q][a][t]), rs, nb), fmaf(bhi(xr[q][a][t]), rs, nb));
        *(LAS v4u*)(L + xo[a] + xT[q] * 1024) = o;
      }
    }
    if (q == 0) { DMA_W(1, 0); DMA_W(1, 1); DMA_W(2, 0); DMA_W(2, 1); }
    if (q == 1) {
      DMA_W(3, 0); DMA_W(3, 1);
#pragma unroll
      for (int e = 0; e < EARLY_W1; ++e) LD_W1(0, e);
      SB();
    }
  }
  const unsigned tag = __builtin_amdgcn_readfirstlane(epoch_v) + 1u;
  asm volatile("" ::"v"(code_v));
  waitv<4 + EARLY_W1>();
  BAR();
  STAMP(2);
  int ov[5];
#pragma unroll
  for (int k = 0; k < 5; ++k) {
    const int q = k * 64 + lane, row = q / 10, ch = q - row * 10;
    ov[k] = swap23(row) * 1280 + c * 160 + ch * 16;
  }
#define DMA_WO(k) do { if ((k) < 10) dma(rWo, ov[(k) % 5], (w + 8 * ((k) / 5)) * (32 * 1280), (w + 8 * ((k) / 5)) * 5120 + ((k) % 5) * 1024); \
    else { const int pc_ = (w & 1) * 3 + (k) - 10; const bool on_ = pc_ < 5; \
      dma(rWo, (w & 1) ? ((k) == 10 ? ov[3] : (k) == 11 ? ov[4] : OOB) : ov[(k) - 10], (16 + (w >> 1)) * (32 * 1280), on_ ? (16 + (w >> 1)) * 5120 + pc_ * 1024 : LDS_BYTES); } } while (0)
  f32x4 acc1[2][5];
#pragma unroll
  for (int sl = 0; sl < 2; ++sl)
#pragma unroll
    for (int T = 0; T < 5; ++T) acc1[sl][T] = f32x4{0.f, 0.f, 0.f, 0.f};
  const int n0 = 5 + w, n1 = w < 5 ? w : (w < 7 ? 8 + w : 14);
  {
    const lptr ar0 = L + (g * 16 + (l16 ^ g)) * 16, ar1 = L + (g * 16 + (l16 ^ (g | 12))) * 16;
    const int trow = 16 * (g >> 1) + 4 * (g & 1) + (l16 >> 2);
    const lptr br0 = L + L_WRING + trow * 480 + n0 * 32 + (l16 & 3) * 8, br1 = L + L_WRING + trow * 480 + n1 * 32 + (l16 & 3) * 8;
    v4u fa[2][5], fb[2][2];
#define RD_FR(kap) do { const int so_ = ((kap) & 3) * 15360; \
      { const v2u t0 = trrd(br0 + so_), t1 = trrd(br0 + so_ + 8 * 480); fb[(kap) & 1][0] = v4u{t0.x, t0.y, t1.x, t1.y}; } \
      { const v2u t0 = trrd(br1 + so_), t1 = trrd(br1 + so_ + 8 * 480); fb[(kap) & 1][1] = v4u{t0.x, t0.y, t1.x, t1.y}; } \
      _Pragma("unroll") for (int T = 0; T < 5; ++T) fa[(kap) & 1][T] = *(LAS v4u*)((((kap) & 1) ? ar1 : ar0) + (kap) * 5120 + T * 1024); } while (0)
    RD_FR(0);
    BAR();
#pragma unroll
    for (int s = 0; s < 20; ++s) {
      if (s + 1 < 20) RD_FR(s + 1);
      SB();
      if (s + 4 < 20) DMA_W(s + 4, 0); else DMA_WO(s - 16);
#pragma unroll
      for (int T = 0; T < 5; ++T) acc1[0][T] = mfma(fb[s & 1][0], fa[s & 1][T], acc1[0][T]);
      SB();
      if (s + 4 < 20) DMA_W(s + 4, 1);
#pragma unroll
      for (int T = 0; T < 5; ++T) acc1[1][T] = mfma(fb[s & 1][1], fa[s & 1][T], acc1[1][T]);
      SB();
      if (s < 2) waitv<4 + EARLY_W1>();
      else if (s < 16) waitv<4>();
      else if (s == 16) waitv<3>();
      else if (s == 17) waitv<2>();
      if (s < 19) BAR();
    }
  }
  STAMP(3);
  DMA_WO(4); DMA_WO(5);
#pragma unroll
  for (int sl = 0; sl < 2; ++sl)
#pragma unroll
    for (int T = 0; T < 5; ++T) {
      const f32x4 v = acc1[sl][T];
      *(LAS v2u*)(L + L_QKV + (T * 16 + l16) * QRS + (sl ? n1 : n0) * 32 + g * 8) = v2u{pk(v[0], v[1]), pk(v[2], v[3])};
    }
  BAR();
  DMA_WO(6); DMA_WO(7);
  {
    const int hd = w & 3, par = w >> 2;
    const lptr qb = L + L_QKV + l16 * QRS + hd * 40 + (g < 3 ? g : 0) * 16;
    const int off0 = par ? 9 : 1, lim0 = ti == 0 ? 0 : (par ? 16 : 8);
    v4u kf[3], qf[2];
    v2u vf[3][2];
#pragma unroll
    for (int t = 0; t < 3; ++t) {
      const int T = t == 0 ? 0 : t + 2 * par;
      const v2u a0 = *(LAS v2u*)(qb + T * 16 * QRS + 160), a1 = *(LAS v2u*)(qb + T * 16 * QRS + 168);
      kf[t] = v4u{a0.x, a0.y, a1.x, a1.y};
      if (t) {
        const v2u q0 = *(LAS v2u*)(qb + T * 16 * QRS), q1 = *(LAS v2u*)(qb + T * 16 * QRS + 8);
        qf[t - 1] = v4u{g < 3 ? q0.x : 0u, g < 3 ? q0.y : 0u, g < 2 ? q1.x : 0u, g < 2 ? q1.y : 0u};
      }
      const lptr vb = L + L_QKV + (T * 16 + 4 * g + (l16 >> 2)) * QRS + 320 + hd * 40 + (l16 & 3) * 8;
      vf[t][0] = trrd(vb);
      vf[t][1] = trrd(vb + 32);
    }
#pragma unroll
    for (int it = 0; it < 2; ++it) {
      const f32x4 z = {0.f, 0.f, 0.f, 0.f};
      const f32x4 sp = mfma(kf[it], qf[it], z), sc = mfma(kf[it + 1], qf[it], z);
      float v[8], mx = -3.0e38f;
#pragma unroll
      for (int r = 0; r < 4; ++r) {
        const int kb = 4 * g + r;
        const bool okp = it == 0 ? ((kb >= l16 + off0) & (kb < lim0)) : kb >= l16 + 9;
        const bool okc = (kb <= l16) & (kb + 7 >= l16);
        v[r] = okp ? sp[r] : -3.0e38f;
        v[4 + r] = okc ? sc[r] : -3.0e38f;
        mx = fmaxf(mx, fmaxf(v[r], v[4 + r]));
      }
      mx = maxg(mx);
      const float cs = 0.22360679774997896f * 1.4426950408889634f;
      float sum = 0.f;
#pragma unroll
      for (int r = 0; r < 8; ++r) { v[r] = v[r] > -1.0e38f ? __builtin_amdgcn_exp2f((v[r] - mx) * cs) : 0.f; sum += v[r]; }
      const float inv = __builtin_amdgcn_rcpf(sumg(sum));
      const v4u pf = {pk(v[0] * inv, v[1] * inv), pk(v[2] * inv, v[3] * inv), pk(v[4] * inv, v[5] * inv), pk(v[6] * inv, v[7] * inv)};
      const f32x4 o0 = mfma(v4u{vf[it][0].x, vf[it][0].y, vf[it + 1][0].x, vf[it + 1][0].y}, pf, z);
      const f32x4 o1 = mfma(v4u{vf[it][1].x, vf[it][1].y, vf[it + 1][1].x, vf[it + 1][1].y}, pf, z);
      if (it == 0) { DMA_WO(8); DMA_WO(9); }
      const int sl = it * 32 + 2 * l16 + par;
      const int j = sl & 3, idx = ((sl >> 3) & 7) | (((sl >> 2) & 1) << 3);
#pragma unroll
      for (int pc = 0; pc < 2; ++pc) {
        const int d = hd * 20 + pc * 16 + 4 * g;
        int a;
        if (d < 64) a = (2 * j + (d >> 5)) * 1024 + (((d & 31) >> 3) * 16 + idx) * 16 + ((d >> 2) & 1) * 8;
        else { const int e = (d - 64) >> 2, gs = ((e & 1) << 1) | (e >> 1); a = (8 + (j >> 1)) * 1024 + (gs * 16 + idx) * 16 + (j & 1) * 8; }
        const f32x4 o = pc ? o1 : o0;
        if (pc == 0 || g == 0) *(LAS v2u*)(L + L_SLAB + a) = v2u{pk(o[0], o[1]), pk(o[2], o[3])};
      }
    }
  }
  BAR();
  STAMP(4);
  {
    const int so = WS_O + (ti * 8 + c) * 10240;
    st128c(*(LAS v4u*)(L + L_SLAB + tid * 16), rWS, tid * 16, so);
    if (w < 2) st128c(*(LAS v4u*)(L + L_SLAB + 8192 + tid * 16), rWS, tid * 16 + 8192, so);
    DRAIN();
    BAR();
    if (tid == 0) set_flag(ws, WS_F1 + (ti * 8 + c) * 128, tag);
    DMA_WO(10); DMA_WO(11); DMA_WO(12);
  }
  STAMP(5);
#pragma unroll
  for (int e = EARLY_W1; e < 8; ++e) LD_W1(0, e);
#pragma unroll
  for (int e = 0; e < 8; ++e) LD_W1(1, e);
  SB();
  const int mh = w >> 2, kq = w & 3, jo = kq >> 1, no = (kq & 1) * 3;
  const int jt = 2 * mh + jo;
  const int stok = 64 * ti + 8 * (l16 & 7) + 4 * (l16 >> 3) + jt;
  v2u xres[3];
#pragma unroll
  for (int nn = 0; nn < 3; ++nn)
    xres[nn] = __builtin_amdgcn_raw_buffer_load_b64(rX, stok * 1280 + c * 160 + g * 8, ((nn + no) % 5) * 32, 0);
  v4u w1f[8][4];
  xpose(w1r[0], w1f[0], w1f[1]);
  xpose(w1r[1], w1f[2], w1f[3]);
  wait_flags(ws, WS_F1 + (ti * 8 + 2 * kq + (lane & 1)) * 128, tag);
  STAMP(6);
  unsigned hpk[3][2];
  {
    v4u of[2][5];
#pragma unroll
    for (int pr = 0; pr < 2; ++pr) {
      const int so = WS_O + (ti * 8 + 2 * kq + pr) * 10240;
#pragma unroll
      for (int u = 0; u < 4; ++u) of[pr][u] = ld128c(rWS, lane * 16, so + (2 * (2 * mh + ((u >> 1) ^ jo)) + (u & 1)) * 1024);
      of[pr][4] = ld128c(rWS, lane * 16, so + (8 + mh) * 1024);
    }
    SB();
    BAR();
    f32x4 acc2[2][5];
#pragma unroll
    for (int jj = 0; jj < 2; ++jj)
#pragma unroll
      for (int nn = 0; nn < 5; ++nn) acc2[jj][nn] = f32x4{0.f, 0.f, 0.f, 0.f};
    const int trow = 16 * (g >> 1) + 4 * (g & 1) + (l16 >> 2);
    const lptr wb = L + (160 * kq + trow) * 160 + (l16 & 3) * 8;
    const lptr wb16 = L + (160 * kq + 64 + 4 * g + (l16 >> 2)) * 160 + (l16 & 3) * 8;
#pragma unroll
    for (int ks = 0; ks < 5; ++ks) {
      v4u b0, b1_;
      if (ks < 4) { b0 = of[ks >> 1][ks & 1]; b1_ = of[ks >> 1][2 + (ks & 1)]; }
      else {
        const v4u A = of[0][4], B = of[1][4];
        b0 = jo ? v4u{A.z, A.w, B.z, B.w} : v4u{A.x, A.y, B.x, B.y};
        b1_ = jo ? v4u{A.x, A.y, B.x, B.y} : v4u{A.z, A.w, B.z, B.w};
      }
#pragma unroll
      for (int nn = 0; nn < 5; ++nn) {
        const int n = (nn + no) % 5;
        v2u t0, t1;
        if (ks < 4) { const lptr p = wb + ((ks >> 1) * 80 + (ks & 1) * 32) * 160 + n * 32; t0 = trrd(p); t1 = trrd(p + 8 * 160); }
        else { const lptr p = wb16 + n * 32; t0 = trrd(p); t1 = trrd(p + 80 * 160); }
        const v4u af = {t0.x, t0.y, t1.x, t1.y};
        acc2[0][nn] = mfma(af, b0, acc2[0][nn]);
        acc2[1][nn] = mfma(af, b1_, acc2[1][nn]);
      }
    }
    STAMP(7);
#pragma unroll
    for (int jj = 0; jj < 2; ++jj)
#pragma unroll
      for (int nn = 0; nn < 5; ++nn) {
        const int n = (nn + no) % 5, j = 2 * mh + (jj ^ jo);
        const int okq = 2 * (jj ^ jo) + (n >= 3), i3 = (kq - okq - 1) & 3;
        if (jj == 0 && nn < 2) continue;
        if (i3 != 3) *(LAS f32x4*)(L + L_RED2 + ((j * 5 + n) * 3 + i3) * 1024 + lane * 16) = acc2[jj][nn];
      }
    BAR();
    float hv[3][4];
    float s1 = 0.f;
    const bool three = !(kq & 1);
#pragma unroll
    for (int nn = 0; nn < 3; ++nn) {
      f32x4 v = acc2[0][nn];
      const lptr p = L + L_RED2 + (jt * 5 + (nn + no) % 5) * 3072 + lane * 16;
      v += *(LAS f32x4*)(p); v += *(LAS f32x4*)(p + 1024); v += *(LAS f32x4*)(p + 2048);
      hpk[nn][0] = pk(v[0] + blo(xres[nn].x), v[1] + bhi(xres[nn].x));
      hpk[nn][1] = pk(v[2] + blo(xres[nn].y), v[3] + bhi(xres[nn].y));
      hv[nn][0] = blo(hpk[nn][0]); hv[nn][1] = bhi(hpk[nn][0]); hv[nn][2] = blo(hpk[nn][1]); hv[nn][3] = bhi(hpk[nn][1]);
      if (nn < 2 || three) s1 += (hv[nn][0] + hv[nn][1]) + (hv[nn][2] + hv[nn][3]);
    }
    s1 = sumg(s1);
    const float mp = s1 * (three ? 1.f / 48.f : 1.f / 32.f);
    float m2 = 0.f;
#pragma unroll
    for (int nn = 0; nn < 3; ++nn)
      if (nn < 2 || three) {
#pragma unroll
        for (int r = 0; r < 4; ++r) { const float d = hv[nn][r] - mp; m2 = fmaf(d, d, m2); }
      }
    m2 = sumg(m2);
    const int sto = WS_ST + (ti * 4 + jt) * 4096;
    if (g == 0) st128c(v4u{asu(mp), tag, asu(m2), tag}, rWS, (c * 16 + l16) * 32 + (kq & 1) * 16, sto);
    STAMP(8);
    v4u sv[4];
#pragma nounroll
    for (int sp = 0; sp < SPIN_CAP; ++sp) {
      bool ok = true;
#pragma unroll
      for (int a = 0; a < 4; ++a) sv[a] = ld128c(rWS, lane * 32 + (a & 1) * 16, sto + (a >> 1) * 2048);
#pragma unroll
      for (int a = 0; a < 4; ++a) ok = ok & (sv[a].y == tag) & (sv[a].w == tag);
      if (__all(ok)) break;
      __builtin_amdgcn_s_sleep(1);
      asm volatile("" ::: "memory");
    }
    STAMP(9);
    float ms = 0.f;
#pragma unroll
    for (int a = 0; a < 4; ++a) ms = fmaf(asf(sv[a].x), (a & 1) ? 32.f : 48.f, ms);
    const float mean = sumg(ms) * (1.f / 640.f);
    float M2 = 0.f;
#pragma unroll
    for (int a = 0; a < 4; ++a) {
      const float d = asf(sv[a].x) - mean;
      M2 += fmaf(d * d, (a & 1) ? 32.f : 48.f, asf(sv[a].z));
    }
    const float var = sumg(M2) * (1.f / 640.f);
    const float rr = __builtin_amdgcn_rsqf(fmaf(1e-5f, var + 1e-5f, var)), nb = -mean * rr;
    const int sl = stok & 63;
#pragma unroll
    for (int nn = 0; nn < 3; ++nn)
      if (nn < 2 || three)
        *(LAS v2u*)(L + L_HN + swap23(sl) * 160 + ((nn + no) % 5) * 32 + g * 8) =
            v2u{pk(fmaf(hv[nn][0], rr, nb), fmaf(hv[nn][1], rr, nb)), pk(fmaf(hv[nn][2], rr, nb), fmaf(hv[nn][3], rr, nb))};
#pragma unroll
    for (int nn = 0; nn < 3; ++nn) asm volatile("" : "+v"(hpk[nn][0]), "+v"(hpk[nn][1]));
    BAR();
#pragma unroll
    for (int k = 0; k < 2; ++k) {
      const int u = w + 8 * k;
      if (k == 1 && w >= 2) break;
      const lptr p = L + L_HN + ((u & 1) * 32 + trow) * 160 + (u >> 1) * 32 + (l16 & 3) * 8;
      const v2u t0 = trrd(p), t1 = trrd(p + 8 * 160);
      st128c(v4u{t0.x, t0.y, t1.x, t1.y}, rWS, lane * 16, WS_HN + ((c * 5 + (u >> 1)) * 64 + 2 * ti + (u & 1)) * 1024);
    }
    DRAIN();
    BAR();
    if (tid == 0) set_flag(ws, WS_F2 + (c * 32 + ti) * 128, tag);
  }
  STAMP(10);
  const int w2v = (32 * (g & 1) + 4 * (g >> 1) + 2 * hi) * 4096 + c8 * 16;
  v4u w2r[3][8];
  if (p3on) {
#pragma unroll
    for (int sg = 2; sg < 4; ++sg)
#pragma unroll
      for (int e = 0; e < 8; ++e) LD_W1(sg, e);
    SB();
    xpose(w1r[2], w1f[4], w1f[5]);
    xpose(w1r[3], w1f[6], w1f[7]);
    const int c0 = (dt * 64) / 80, c1 = (dt * 64 + 63) / 80;
    wait_flags(ws, WS_F2 + (((lane & 4) ? c1 : c0) * 32 + w * 4 + (lane & 3)) * 128, tag);
    STAMP(11);
    v4u hf[4][8];
#pragma unroll
    for (int sg = 0; sg < 3; ++sg)
#pragma unroll
      for (int k = 0; k < 8; ++k) hf[sg][k] = ld128c(rWS, lane * 16, WS_HN + ((dt * 4 + (((w >> 1) + 1 + sg) & 3)) * 64 + w * 8 + ((k + 2 * rot3) & 7)) * 1024);
    SB();
    f32x4 own0, own1;
#pragma unroll
    for (int sg = 0; sg < 4; ++sg) {
      f32x4 a3[4];
#pragma unroll
      for (int j = 0; j < 4; ++j) a3[j] = f32x4{0.f, 0.f, 0.f, 0.f};
#pragma unroll
      for (int k = 0; k < 8; ++k) {
#pragma unroll
        for (int j = 0; j < 4; ++j) a3[j] = mfma(w1f[k][j], hf[sg][k], a3[j]);
        if (sg == 0) { hf[3][k] = ld128c(rWS, lane * 16, WS_HN + ((dt * 4 + (w >> 1)) * 64 + w * 8 + ((k + 2 * rot3) & 7)) * 1024); SB(); }
      }
      const int m = ((w >> 1) + 1 + sg) & 3;
      const lptr p = L + ((m * 4) * 8 + w) * 1024 + lane * 16;
      if (sg < 3) {
#pragma unroll
        for (int j = 0; j < 4; ++j) *(LAS f32x4*)(p + j * 8192) = a3[j];
      } else {
        const bool od = w & 1;
        own0 = od ? a3[2] : a3[0]; own1 = od ? a3[3] : a3[1];
        *(LAS f32x4*)(p + (od ? 0 : 2) * 8192) = od ? a3[0] : a3[2];
        *(LAS f32x4*)(p + (od ? 1 : 3) * 8192) = od ? a3[1] : a3[3];
      }
    }
    STAMP(12);
    unsigned b1v[4];
#pragma unroll
    for (int r = 0; r < 4; ++r) b1v[r] = *(const unsigned*)(b1 + 64 * ft + 32 * (g & 1) + 8 * r + 4 * (g >> 1) + 2 * (w & 1));
#pragma unroll
    for (int sg = 0; sg < 3; ++sg)
#pragma unroll
      for (int e = 0; e < 8; ++e) w2r[sg][e] = ld128(rW2, w2v, (192 * w + 64 * sg + 8 * (e >> 1) + (e & 1)) * 4096 + ti * 128);
    SB();
    BAR();
    {
      const lptr p = L + (((w >> 1) * 4 + 2 * (w & 1)) * 8) * 1024 + lane * 16;
#pragma unroll
      for (int k = 1; k < 8; ++k) {
        const int ww = (w + k) & 7;
        own0 += *(LAS f32x4*)(p + ww * 1024);
        own1 += *(LAS f32x4*)(p + 8192 + ww * 1024);
      }
      v4u uo;
#pragma unroll
      for (int r = 0; r < 4; ++r) {
        float gl[2];
#pragma unroll
        for (int t = 0; t < 2; ++t) {
          const float v = (t ? own1[r] + bhi(b1v[r]) : own0[r] + blo(b1v[r]));
          const float z = v * fmaf(v * v, 0.044715f * 0.7978845608028654f, 0.7978845608028654f);
          gl[t] = v * __builtin_amdgcn_rcpf(1.f + __builtin_amdgcn_exp2f(z * -2.885390081777927f));
        }
        uo[r] = pk(gl[0], gl[1]);
      }
      st128c(uo, rWS, lane * 16, WS_U + ((dt * 4 + (w >> 1)) * 48 + 2 * ft + (w & 1)) * 1024);
    }
    DRAIN();
    BAR();
    if (tid == 0) set_flag(ws, WS_F3 + (dt * 24 + ft) * 128, tag);
  } else {
#pragma unroll
    for (int sg = 0; sg < 3; ++sg)
#pragma unroll
      for (int e = 0; e < 8; ++e) w2r[sg][e] = ld128(rW2, w2v, (192 * w + 64 * sg + 8 * (e >> 1) + (e & 1)) * 4096 + ti * 128);
  }
  STAMP(13);
  {
    const float b2v = blo(b2[stok]);
    v4u w2f[6][4];
    xpose(w2r[0], w2f[0], w2f[1]);
    xpose(w2r[1], w2f[2], w2f[3]);
    xpose(w2r[2], w2f[4], w2f[5]);
    const int d0 = (c * 80) / 64, d1 = (c * 80 + 79) / 64, l3 = (lane & 3) == 3 ? 0 : (lane & 3);
    wait_flags(ws, WS_F3 + (((lane & 4) ? d1 : d0) * 24 + w * 3 + l3) * 128, tag);
    STAMP(14);
    v4u uf[5][6];
#pragma unroll
    for (int n = 0; n < 5; ++n)
#pragma unroll
      for (int k = 0; k < 6; ++k) uf[n][k] = ld128c(rWS, lane * 16, WS_U + ((c * 5 + n) * 48 + w * 6 + k) * 1024);
    SB();
#pragma unroll
    for (int n = 0; n < 5; ++n) {
      f32x4 a4[4];
#pragma unroll
      for (int j = 0; j < 4; ++j) a4[j] = f32x4{0.f, 0.f, 0.f, 0.f};
#pragma unroll
      for (int k = 0; k < 6; ++k)
#pragma unroll
        for (int j = 0; j < 4; ++j) a4[j] = mfma(uf[n][k], w2f[k][j], a4[j]);
#pragma unroll
      for (int j = 0; j < 4; ++j) *(LAS f32x4*)(L + ((n * 4 + j) * 8 + w) * 1024 + lane * 16) = a4[j];
    }
    STAMP(15);
    BAR();
#pragma unroll
    for (int nn = 0; nn < 3; ++nn) {
      if (nn == 2 && (kq & 1)) break;
      const int n = nn + no;
      const lptr p = L + ((n * 4 + jt) * 8) * 1024 + lane * 16;
      f32x4 v = *(LAS f32x4*)(p);
#pragma unroll
      for (int k = 1; k < 8; ++k) v += *(LAS f32x4*)(p + k * 1024);
      const v2u o = {pk(v[0] + b2v + blo(hpk[nn][0]), v[1] + b2v + bhi(hpk[nn][0])), pk(v[2] + b2v + blo(hpk[nn][1]), v[3] + b2v + bhi(hpk[nn][1]))};
      *(v2u*)(out + stok * 640 + c * 80 + n * 16 + g * 4) = o;
    }
  }
  if (b == 0 && tid == 0) set_flag(ws, WS_EPOCH, tag);
  STAMP(16);
  asm volatile(".Lk_end:");
}

extern "C" void kernel_launch(void* const* d_in, const int* in_sizes, int n_in, void* d_out, int out_size, void* d_ws,
                              size_t ws_size, hipStream_t stream) {
  (void)hipFuncSetAttribute((const void*)fused_kernel, hipFuncAttributeMaxDynamicSharedMemorySize, LDS_BYTES);
  fused_kernel<<<256, 512, LDS_BYTES, stream>>>((const u16*)d_in[0], (const u16*)d_in[1], (const u16*)d_in[2], (const u16*)d_in[4],
                                                (const u16*)d_in[5], (const u16*)d_in[6], (const u16*)d_in[7], (u16*)d_out, (char*)d_ws
  );
}
```

```cpp
#include <hip/hip_runtime.h>
#include <hip/hip_bf16.h>

typedef unsigned short u16;
typedef unsigned v4u __attribute__((ext_vector_type(4)));
typedef unsigned v2u __attribute__((ext_vector_type(2)));
typedef float f32x4 __attribute__((ext_vector_type(4)));
typedef float f32x2 __attribute__((ext_vector_type(2)));
typedef short bf16x8 __attribute__((ext_vector_type(8)));
typedef short s16x4 __attribute__((ext_vector_type(4)));
typedef __bf16 bf2 __attribute__((ext_vector_type(2)));
#define LAS __attribute__((address_space(3)))
#define GAS __attribute__((address_space(1)))
typedef LAS char* lptr;
typedef __amdgpu_buffer_rsrc_t rsrc_t;

#define BAR() asm volatile("s_waitcnt lgkmcnt(0)\n\ts_barrier" ::: "memory")
#define DRAIN() asm volatile("s_waitcnt vmcnt(0)" ::: "memory")
#define SB() __builtin_amdgcn_sched_barrier(0)
#define DEV __device__ __forceinline__

constexpr int SC1 = 16;
constexpr int OOB = 0x7ffffff0;
constexpr int WS_EPOCH = 0;
constexpr int WS_F1 = 524288, WS_F2 = WS_F1 + 131072, WS_F3 = WS_F2 + 131072;
constexpr int WS_ST = 131072;
constexpr int WS_O = 1 << 20;
constexpr int WS_HN = 4 << 20;
constexpr int WS_U = 8 << 20;
constexpr int WS_BYTES = 12 << 20;
constexpr int LDS_BYTES = 163840;
constexpr int L_QKV = 0, QRS = 488;
constexpr int L_STG = 0;
constexpr int L_HN = 32768;
constexpr int L_HB = 44288;
constexpr int L_FLAG = 39040;
constexpr int L_W1A = 49152, L_W1B = 98304;
constexpr int L_RED2 = 102400;
constexpr int L_XR = 49152;
constexpr int L_WO = 61440;
constexpr int L_ST = L_WO + 19 * 5120;
constexpr int L_W2A = 147456;
constexpr int L_PA = 98304;
constexpr int WO_PER = 4;
constexpr int T1S = 13, T2S = 17;
constexpr int PKS = 2, DS = 12, D2 = 8;
constexpr int SPIN_CAP = 1 << 16;

DEV v4u ld128c(rsrc_t r, int v, int s) { return __builtin_amdgcn_raw_buffer_load_b128(r, v, s, SC1); }
DEV void st128c(v4u d, rsrc_t r, int v, int s) {
  __builtin_amdgcn_raw_buffer_store_b128(d, r, v, s, SC1);
  asm volatile("s_nop 1" ::"v"(d));
}
DEV f32x4 mfma(v4u a, v4u b, f32x4 c) {
  return __builtin_amdgcn_mfma_f32_16x16x32_bf16(__builtin_bit_cast(bf16x8, a), __builtin_bit_cast(bf16x8, b), c, 0, 0, 0);
}
DEV unsigned pk(float lo, float hi) { bf2 v; v[0] = (__bf16)lo; v[1] = (__bf16)hi; return __builtin_bit_cast(unsigned, v); }
DEV float asf(unsigned u) { return __builtin_bit_cast(float, u); }
DEV unsigned asu(float f) { return __builtin_bit_cast(unsigned, f); }
DEV float blo(unsigned u) { return __builtin_bit_cast(float, u << 16); }
DEV float bhi(unsigned u) { return __builtin_bit_cast(float, u & 0xffff0000u); }
DEV v2u trrd(lptr p) { return __builtin_bit_cast(v2u, __builtin_amdgcn_ds_read_tr16_b64_v4i16((LAS s16x4*)p)); }
template <int CTRL> DEV float dppf(float x) {
  return __builtin_bit_cast(float, __builtin_amdgcn_update_dpp(0, __builtin_bit_cast(int, x), CTRL, 0xF, 0xF, true));
}
DEV float sumg(float x) {
  auto r = __builtin_amdgcn_permlane16_swap(asu(x), asu(x), false, false);
  x = asf(r[0]) + asf(r[1]);
  auto q = __builtin_amdgcn_permlane32_swap(asu(x), asu(x), false, false);
  return asf(q[0]) + asf(q[1]);
}
#define dma(r, voff, soff, ldsbase, imm) \
  asm volatile("s_add_i32 m0, %0, %4\n\ts_nop 0\n\tbuffer_load_dwordx4 %1, %2, %3 offen lds" ::"s"(ldsbase), "v"(voff), "s"(r), "s"(soff), "i"(imm) : "memory", "scc")
template <int N> DEV void waitv() { asm volatile("s_waitcnt vmcnt(%0)" ::"i"(N) : "memory"); }
DEV int swap23(int r) { return (r & ~12) | ((r & 4) << 1) | ((r & 8) >> 1); }

template <int DELAY, bool ATOM> DEV void wait_flags(char* ws, int off, bool on, unsigned tag, unsigned zero) {
  GAS unsigned* p = (GAS unsigned*)(ws + off);
  if (DELAY) __builtin_amdgcn_s_sleep(DELAY);
#pragma nounroll
  for (int sp = 0; sp < SPIN_CAP; ++sp) {
    unsigned v = tag;
    if (on) v = ATOM ? __hip_atomic_fetch_add(p, zero, __ATOMIC_RELAXED, __HIP_MEMORY_SCOPE_AGENT)
                     : __hip_atomic_load(p, __ATOMIC_RELAXED, __HIP_MEMORY_SCOPE_AGENT);
    if (__all(v == tag)) break;
    __builtin_amdgcn_s_sleep(1);
  }
  __builtin_amdgcn_fence(__ATOMIC_ACQUIRE, "wavefront");
}
template <int I> struct IC { static constexpr int v = I; };
template <int I, int N, class F> DEV void sfor(F&& f) { if constexpr (I < N) { f(IC<I>{}); sfor<I + 1, N>(f); } }
#define max2(a, b) __builtin_amdgcn_fmed3f(a, b, finf)
constexpr float CS = 0.22360679774997896f * 1.4426950408889634f;
DEV v4u cat(v2u a, v2u b) { return v4u{a.x, a.y, b.x, b.y}; }
#define LGKM0() asm volatile("s_waitcnt lgkmcnt(0)" ::: "memory")
DEV void set_flag(char* ws, int off, unsigned tag) {
  __hip_atomic_store((GAS unsigned*)(ws + off), tag, __ATOMIC_RELAXED, __HIP_MEMORY_SCOPE_AGENT);
}

__global__ void __launch_bounds__(512, 2)
fused_kernel(const u16* __restrict__ x, const u16* __restrict__ wqkv, const u16* __restrict__ wo,
             const u16* __restrict__ w1, const u16* __restrict__ b1, const u16* __restrict__ w2,
             const u16* __restrict__ b2, u16* __restrict__ out, char* ws) {
  extern __shared__ __attribute__((aligned(16))) char smem[];
  const lptr L = (lptr)smem;
  const int tid = threadIdx.x, lane = tid & 63, w = __builtin_amdgcn_readfirstlane(tid >> 6);
  const int l16 = lane & 15, g = lane >> 4;
  const int b = blockIdx.x, xcd = b & 7, jb = b >> 3, xi = xcd >> 1, xc = xcd & 1;
  const int ti = xi * 8 + (jb >> 2), c = xc * 4 + (jb & 3);
  const int dt = xc * 5 + jb % 5, ft = xi * 6 + jb / 5;
  const bool p3on = jb < 30;

  unsigned code_v;
  {
    unsigned long long pc;
    int len;
    asm volatile("s_getpc_b64 %0\n.Lk_begin:\n\ts_mov_b32 %1, .Lk_end-.Lk_begin" : "=s"(pc), "=s"(len));
    const rsrc_t rC = __builtin_amdgcn_make_buffer_rsrc((void*)pc, 0, len, 0x00020000);
    asm volatile("s_nop 4\n\tbuffer_load_dword %0, %1, %2, %3 offen" : "=v"(code_v) : "v"(lane * 4), "s"(rC), "s"((jb * 8 + w) * 256));
  }
  const int jt = w & 3;
  const int rot2 = (((xi * 5) >> 1) + ti % 3 + ((c * 5) >> 2)) % 10;
#define KPAIR(p) (((p) + rot2) >= 10 ? (p) + rot2 - 10 : (p) + rot2)
#define WPIECE(n) ({ const unsigned q_ = (n) * 64 + lane, row_ = __umul24(q_, 2185u) >> 16, cc_ = q_ - __umul24(row_, 30u), seg_ = __umul24(cc_, 26u) >> 8; \
    (int)(__umul24((unsigned)swap23(row_), 3840u) + __umul24(seg_, 1120u) + cc_ * 16u) + c * 160; })
#define XPIECE(n) ({ const int T_ = (n) >> 1, r_ = 8 * ((n) & 1) + (lane >> 3); \
    int s_ = T_ == 0 ? 64 * ti - 16 - 15 * ((n) & 1) + 2 * r_ : 64 * ti + ((T_ - 1) & 1) * 32 + 2 * r_ + ((T_ - 1) >> 1); \
    s_ = s_ < 0 ? 0 : s_; s_ * 1280 + (((lane & 7) ^ ((r_ >> 1) & 7)) * 16); })
  const bool w67 = w >= 6;
  const int pl = w * 1024;
  int pv[5];
  pv[4] = XPIECE(w + 2);
  pv[0] = WPIECE(w);
  pv[1] = WPIECE(w + 8);
  pv[2] = WPIECE(w + 16);
  { const int xa_ = XPIECE(w & 1), wa_ = WPIECE(w + 24); pv[3] = w67 ? xa_ : wa_; }
  asm volatile("" : "+v"(pv[0]), "+v"(pv[1]), "+v"(pv[2]), "+v"(pv[3]), "+v"(pv[4]));
  SB();
  const rsrc_t rX = __builtin_amdgcn_make_buffer_rsrc((void*)x, 0, 2048 * 640 * 2, 0x00020000);
  const rsrc_t rWq = __builtin_amdgcn_make_buffer_rsrc((void*)wqkv, 0, 640 * 1920 * 2, 0x00020000);
  const rsrc_t rB = __builtin_amdgcn_make_buffer_rsrc((void*)(w67 ? x : wqkv), 0, w67 ? 2048 * 640 * 2 : 640 * 1920 * 2, 0x00020000);
  const int bstep = w67 ? 128 : 64 * 3840;
#define DMA_P(P, k) dma((k) == 4 ? rX : (k) == 3 ? rB : rWq, pv[k], KPAIR(P) * ((k) == 4 ? 128 : (k) == 3 ? bstep : 64 * 3840), pl, ((P) & 3) * 40960 + (k) * 8192)
#define DMA_PAIR(P) do { DMA_P(P, 4); DMA_P(P, 0); DMA_P(P, 1); DMA_P(P, 3); DMA_P(P, 2); } while (0)
  unsigned epoch_v;
  asm volatile("global_load_dword %0, %1, %2 sc1" : "=v"(epoch_v) : "v"(WS_EPOCH), "s"(ws) : "memory");
  __builtin_amdgcn_s_setprio(3);
  DMA_PAIR(0);
  __builtin_amdgcn_s_setprio(2);
  DMA_PAIR(1);
  __builtin_amdgcn_s_setprio(0);
  asm volatile("" : "+s"(b1), "+s"(b2), "+s"(out), "+s"(w1), "+s"(w2), "+s"(wo), "+s"(ws));
  const rsrc_t rWo = __builtin_amdgcn_make_buffer_rsrc((void*)wo, 0, 640 * 640 * 2, 0x00020000);
  const rsrc_t rW1 = __builtin_amdgcn_make_buffer_rsrc((void*)w1, 0, 2048 * 1536 * 2, 0x00020000);
  const rsrc_t rW2 = __builtin_amdgcn_make_buffer_rsrc((void*)w2, 0, 1536 * 2048 * 2, 0x00020000);
  const rsrc_t rWS = __builtin_amdgcn_make_buffer_rsrc((void*)ws, 0, WS_BYTES, 0x00020000);
  int ov[5];
#pragma unroll
  for (int k = 0; k < 5; ++k) {
    const unsigned q = k * 64 + lane, row = __umul24(q, 6554u) >> 16;
    ov[k] = (int)(__umul24(row, 1120u) + q * 16u) + c * 160;
  }
#define DMA_WO(grp, k) dma(rWo, ov[k], (grp) * (32 * 1280), (grp) * 5120, L_WO + (k) * 1024)
  f32x4 acc1[2][5];
#pragma unroll
  for (int sl = 0; sl < 2; ++sl)
#pragma unroll
    for (int T = 0; T < 5; ++T) acc1[sl][T] = f32x4{0.f, 0.f, 0.f, 0.f};
  const int n0 = 5 + w, n1 = w < 5 ? w : (w < 7 ? 8 + w : 14);
  const int trow = 16 * (g >> 1) + 4 * (g & 1) + (l16 >> 2);
  float finf = __builtin_inff();
  asm volatile("" : "+v"(finf));
  f32x4 biasP, biasC;
  v4u qmask, pmask;
  {
    const int off0 = jt ? 9 : 1, lim0 = (jt & 1) ? 16 : (ti == 0 ? 0 : (jt ? 16 : 8));
    unsigned mc[4];
#pragma unroll
    for (int r = 0; r < 4; ++r) {
      const int kb = 4 * g + r;
      biasC[r] = ((kb <= l16) & (kb + 7 >= l16)) ? 0.f : -1.0e30f;
      biasP[r] = ((kb >= l16 + off0) & (kb < lim0)) ? 0.f : -1.0e30f;
      mc[r] = kb <= l16 ? ((r & 1) ? 0xffff0000u : 0xffffu) : 0u;
    }
    pmask = v4u{~(mc[0] | mc[1]), ~(mc[2] | mc[3]), mc[0] | mc[1], mc[2] | mc[3]};
    const unsigned q0 = g < 3 ? 0xffffffffu : 0u, q1 = g < 2 ? 0xffffffffu : 0u;
    qmask = v4u{q0, q0, q1, q1};
    asm volatile("" : "+v"(biasP), "+v"(biasC), "+v"(qmask), "+v"(pmask));
  }
  unsigned tdum = 0u;
  const int tvo = (w < 5 && lane < 5) ? (xi * 160 + jb * 5 + w) * 1280 + xc * 640 + lane * 128 : OOB;
  const int tv1 = (lane < 32 && (lane & 7) < 6) ? (jb * 64 + xc * 32 + w * 4 + (lane >> 3)) * 3072 + (xi * 6 + (lane & 7)) * 128 : OOB;
  const int tv2 = lane < 24 ? (jb * 48 + xc * 24 + w * 3 + (lane >> 3)) * 4096 + (xi * 8 + (lane & 7)) * 128 : OOB;
#define TOUCH(v, r) asm volatile("buffer_load_dword %0, %1, %2, 0 offen" : "+v"(tdum) : "v"(v), "s"(r) : "memory")
  unsigned tag;
  v2u st1[5];
  {
    const int ab = 30720 + (l16 >> 3) * 1024 + (l16 & 7) * 128;
    const lptr ar0 = L + ab + ((g ^ ((l16 >> 1) & 7)) * 16), ar1 = L + ab + (((4 + g) ^ ((l16 >> 1) & 7)) * 16);
    const int To = w < 5 ? w : w - 5;
    const lptr or0 = ar0 + To * 2048, or1 = ar1 + To * 2048;
    const lptr br0 = L + trow * 480 + n0 * 32 + (l16 & 3) * 8, br1 = L + trow * 480 + n1 * 32 + (l16 & 3) * 8;
    v4u fa[2][5], fo[2];
    v2u fb[2][4];
    f32x4 s1 = {0.f, 0.f, 0.f, 0.f}, s2 = {0.f, 0.f, 0.f, 0.f};
    const v4u ones = {0x3f803f80u, 0x3f803f80u, 0x3f803f80u, 0x3f803f80u};
#define RD1(kap, i) do { if ((i) < 4) fb[(kap) & 1][(i) & 3] = trrd((((i) & 2) ? br1 : br0) + (((kap) >> 1) & 3) * 40960 + ((kap) & 1) * 15360 + ((i) & 1) * (8 * 480)); \
      else fa[(kap) & 1][((i) + 1) % 5] = *(LAS v4u*)((((kap) & 1) ? ar1 : ar0) + (((kap) >> 1) & 3) * 40960 + (((i) + 1) % 5) * 2048); } while (0)
#define RDO(kap) fo[(kap) & 1] = *(LAS v4u*)((((kap) & 1) ? or1 : or0) + (((kap) >> 1) & 3) * 40960)
#define STAT(kap) do { s1 = mfma(ones, fo[(kap) & 1], s1); s2 = mfma(fo[(kap) & 1], fo[(kap) & 1], s2); } while (0)
    asm volatile("" ::"v"(ar0), "v"(ar1), "v"(br0), "v"(br1), "v"(or0), "v"(or1), "v"(ov[0]), "v"(ov[1]), "v"(ov[2]), "v"(ov[3]), "v"(ov[4]), "v"(tv1), "v"(tv2), "v"(tvo));
    DMA_PAIR(2);
    TOUCH(tvo, rWo);
    waitv<6>();
    asm volatile("" : "+v"(epoch_v) : "v"(code_v));
    tag = __builtin_amdgcn_readfirstlane(epoch_v) + 1u;
    BAR();
#pragma unroll
    for (int i = 0; i < 9; ++i) RD1(0, i);
    RDO(0); RDO(1);
    STAT(0); STAT(1);
    SB();
    sfor<0, 20>([&](auto S_) __attribute__((always_inline)) {
      constexpr int s = decltype(S_)::v, P = s >> 1;
      const v4u b0 = cat(fb[s & 1][0], fb[s & 1][1]), b1_ = cat(fb[s & 1][2], fb[s & 1][3]);
      const v4u f0 = fa[s & 1][0];
      const v4u a0 = {l16 == 0 ? 0x3f803f80u : f0.x, l16 == 0 ? 0x3f803f80u : f0.y, l16 == 0 ? 0x3f803f80u : f0.z, l16 == 0 ? 0x3f803f80u : f0.w};
#pragma unroll
      for (int i = 0; i < 10; ++i) {
        const int T = (i + 1) % 5;
        acc1[i / 5][T] = mfma(i < 5 ? b0 : b1_, T ? fa[s & 1][T] : a0, acc1[i / 5][T]);
        {
          const int sl_ = (s & 1) * 10 + i;
          if (sl_ % 3 == 0 && sl_ <= 12) {
            const int k = sl_ / 3;
            if (P + 3 < 10) DMA_P(P + 3, k == 0 ? 4 : k == 1 ? 0 : k == 2 ? 1 : k == 3 ? 3 : 2);
            else if (k < WO_PER) { const int q = (P - 7) * WO_PER + k; if (q < 5) DMA_WO(4 + w, q % 5); else if (q < 10 && w < 7) DMA_WO(12 + w, q % 5); }
          }
        }
        if (s == T1S && i == 9) TOUCH(tv1, rW1);
        if (s == T2S && i == 9) TOUCH(tv2, rW2);
        if (s + 2 < 20 && i == 0) RDO(s + 2);
        if (s + 1 < 20 && i < 9) RD1(s + 1, i);
        if (s + 2 < 20 && i == 7 && w < 5) STAT(s + 2);
        if (s == 17 && i == 9) {
          const float mu = s1[0] * (1.f / 640.f);
          const float q2 = (l16 & 2) ? ((l16 & 1) ? s2[3] : s2[2]) : ((l16 & 1) ? s2[1] : s2[0]);
          const float var = fmaxf(q2 * (1.f / 640.f) - mu * mu, 0.f);
          const float rs = __builtin_amdgcn_rsqf(var + 1e-5f);
          if (g == (l16 >> 2) && w < 5) *(LAS v2u*)(L + L_ST + (To * 16 + l16) * 8) = v2u{asu(-mu), asu(rs)};
        }
        if (s == 19 && i < 5) st1[i] = *(LAS v2u*)(L + L_ST + (i * 16 + l16) * 8);
        SB();
      }
      if (s & 1) {
        if (P < 7) waitv<5 + (P == 0) + (s == T1S) + (s == T2S) + (s - 2 == T1S) + (s - 2 == T2S)>();
        else if (P == 7) waitv<WO_PER + (s == T1S) + (s == T2S) + (s - 2 == T1S) + (s - 2 == T2S)>();
        if (s < 19) BAR();
      }
    });
  }
#pragma unroll
  for (int sl = 0; sl < 2; ++sl) {
    const f32x2 cs0 = {dppf<0x150>(acc1[sl][0][0]), dppf<0x150>(acc1[sl][0][1])}, cs1 = {dppf<0x150>(acc1[sl][0][2]), dppf<0x150>(acc1[sl][0][3])};
#pragma unroll
    for (int T = 0; T < 5; ++T) {
      const f32x4 v = acc1[sl][T];
      const f32x2 nm = {asf(st1[T].x), asf(st1[T].x)}, rs = {asf(st1[T].y), asf(st1[T].y)};
      const f32x2 y0 = __builtin_elementwise_fma(cs0, nm, f32x2{v[0], v[1]}) * rs, y1 = __builtin_elementwise_fma(cs1, nm, f32x2{v[2], v[3]}) * rs;
      *(LAS v2u*)(L + L_QKV + (T * 16 + l16) * QRS + (sl ? n1 : n0) * 32 + g * 8) = v2u{pk(y0[0], y0[1]), pk(y1[0], y1[1])};
    }
  }
  const int sloc = (jt & 1) * 32 + 2 * l16 + (jt >> 1);
  const int stok = 64 * ti + sloc;
  const int rot3 = jb % 5, rot4 = c & 3;
  unsigned zero = 0u;
  asm volatile("" : "+v"(zero));
  const int pch = ((lane & 7) ^ ((lane >> 3) & 6)) * 16;
  const int w1v = p3on ? (16 * (lane >> 5) + 2 * ((lane >> 3) & 3)) * 3072 + pch : OOB;
  const int w2v = (lane >> 3) * 4096 + pch;
#define K8(k) (((k) + 2 * rot3) & 7)
#define DMA_W1(wv_, k, base, imm) _Pragma("unroll") for (int i_ = 0; i_ < 4; ++i_) \
    dma(rW1, w1v, (256 * (wv_) + 64 * (K8(k) >> 1) + ((k) & 1) + 32 * (i_ >> 1) + 8 * (i_ & 1)) * 3072 + ft * 128, base, (imm) + i_ * 1024)
#define K6(k) ({ int q_ = (k) + 2 * rot4; q_ = q_ >= 6 ? q_ - 6 : q_; q_ >= 6 ? q_ - 6 : q_; })
#define DMA_W2(wv_, k, base, imm) _Pragma("unroll") for (int i_ = 0; i_ < 4; ++i_) \
    dma(rW2, w2v, (192 * (wv_) + 32 * K6(k) + 16 * (i_ & 1) + 8 * (i_ >> 1)) * 4096 + ti * 128, base, (imm) + i_ * 1024)
  int tro[4];
#pragma unroll
  for (int j = 0; j < 4; ++j) { const int r8 = 4 * (g & 1) + (l16 >> 2); tro[j] = (g >> 1) * 2048 + r8 * 128 + ((j ^ (r8 >> 1)) * 32) + (l16 & 3) * 8; }
#define TR_K(dst, base) _Pragma("unroll") for (int j_ = 0; j_ < 4; ++j_) dst[j_] = cat(trrd(L + tro[j_] + (base)), trrd(L + tro[j_] + (base) + 1024))
#define PIN4(f) asm volatile("" : "+v"(f[0]), "+v"(f[1]), "+v"(f[2]), "+v"(f[3]))
#define LFLAG_SET(i) do { if (lane == 0) *(volatile LAS unsigned*)(L + L_FLAG + (i) * 4) = 1u; } while (0)
#define LFLAG_WAIT(i0, mask) do { _Pragma("nounroll") for (int sp_ = 0; sp_ < (SPIN_CAP << 4); ++sp_) { \
      if (__all(*(volatile LAS unsigned*)(L + L_FLAG + ((i0) + (lane & (mask))) * 4) != 0u)) break; \
      __builtin_amdgcn_s_sleep(1); } asm volatile("" ::: "memory"); } while (0)
  const rsrc_t rR = __builtin_amdgcn_make_buffer_rsrc((void*)(w < 5 ? wo : x), 0, w < 5 ? 640 * 640 * 2 : 2048 * 640 * 2, 0x00020000);
  const int rgrp = w < 4 ? w : 19;
  const int rsoff = w < 5 ? rgrp * (32 * 1280) : (64 * ti + 32 * (w - 5)) * 1280, rlds = w < 5 ? L_WO + rgrp * 5120 : L_XR + (w - 5) * 5120;
  v4u w1f[8][4], w2f[6][4];
  unsigned hpk[3][2];
  if (tid < 32) *(LAS unsigned*)(L + L_FLAG + tid * 4) = 0u;
  BAR();
  {
    const int hp = w >> 2, Tq = jt + 1, Tp = (jt & 1) ? jt : 0;
    const lptr qb = L + L_QKV + l16 * QRS + (g < 3 ? g : 0) * 16 + hp * 80;
    const lptr vb = L + L_QKV + (4 * g + (l16 >> 2)) * QRS + 320 + (l16 & 3) * 8;
    v4u kp[2], kc[2], qf[2], va[2], vt[2];
#pragma unroll
    for (int h = 0; h < 2; ++h) {
      const lptr pq = qb + Tq * 16 * QRS + h * 40, pp = qb + Tp * 16 * QRS + h * 40;
      qf[h] = cat(*(LAS v2u*)(pq), *(LAS v2u*)(pq + 8));
      kp[h] = cat(*(LAS v2u*)(pp + 160), *(LAS v2u*)(pp + 168));
      kc[h] = cat(*(LAS v2u*)(pq + 160), *(LAS v2u*)(pq + 168));
    }
#pragma unroll
    for (int h = 0; h < 2; ++h) {
      va[h] = cat(trrd(vb + Tp * 16 * QRS + hp * 80 + h * 40), trrd(vb + Tq * 16 * QRS + hp * 80 + h * 40));
      vt[h] = cat(trrd(vb + Tp * 16 * QRS + hp * 64 + h * 32 + 32), trrd(vb + Tq * 16 * QRS + hp * 64 + h * 32 + 32));
    }
    SB();
    if (w < 7) {
#pragma unroll
      for (int k = 0; k < 5; ++k) dma(rR, ov[k], rsoff, rlds, k * 1024);
    }
    const f32x4 z = {0.f, 0.f, 0.f, 0.f};
    f32x4 sp[2], sc[2];
#pragma unroll
    for (int h = 0; h < 2; ++h) { qf[h] &= qmask; sp[h] = mfma(kp[h], qf[h], biasP); sc[h] = mfma(kc[h], qf[h], biasC); }
    float v[2][4], mx[2];
#pragma unroll
    for (int h = 0; h < 2; ++h) {
#pragma unroll
      for (int r = 0; r < 4; ++r) v[h][r] = max2(sp[h][r], sc[h][r]);
      mx[h] = max2(max2(v[h][0], v[h][1]), max2(v[h][2], v[h][3]));
    }
    {
      auto a01 = __builtin_amdgcn_permlane16_swap(asu(mx[0]), asu(mx[1]), false, false);
      const float m01 = max2(asf(a01[0]), asf(a01[1]));
      auto bq = __builtin_amdgcn_permlane32_swap(asu(m01), asu(m01), false, false);
      const float mm = max2(asf(bq[0]), asf(bq[1])) * -CS;
      auto d0 = __builtin_amdgcn_permlane16_swap(asu(mm), asu(mm), false, false);
      mx[0] = asf(d0[0]); mx[1] = asf(d0[1]);
    }
    const v4u ones = {0x3f803f80u, 0x3f803f80u, 0x3f803f80u, 0x3f803f80u};
    f32x4 o0[2], sm[2], ot = z;
#pragma unroll
    for (int h = 0; h < 2; ++h) {
#pragma unroll
      for (int r = 0; r < 4; ++r) v[h][r] = __builtin_amdgcn_exp2f(fmaf(v[h][r], CS, mx[h]));
      const unsigned p0 = pk(v[h][0], v[h][1]), p1 = pk(v[h][2], v[h][3]);
      const v4u pf = v4u{p0, p1, p0, p1} & pmask;
      const unsigned tm = (l16 >> 2) == 2 * hp + h ? 0xffffffffu : 0u;
      sm[h] = mfma(ones, pf, z);
      o0[h] = mfma(va[h], pf, z);
      ot = mfma(vt[h] & v4u{tm, tm, tm, tm}, pf, ot);
    }
    const float i0 = __builtin_amdgcn_rcpf(sm[0][0]), i1 = __builtin_amdgcn_rcpf(sm[1][0]);
    o0[0] *= i0; o0[1] *= i1; ot *= (g & 1) ? i1 : i0;
    const int so = WS_O + (ti * 8 + c) * 10240 + jt * 2560;
    st128c(v4u{pk(o0[0][0], o0[0][1]), pk(o0[0][2], o0[0][3]), pk(o0[1][0], o0[1][1]), pk(o0[1][2], o0[1][3])}, rWS, lane * 16, so + hp * 1024);
    __builtin_amdgcn_raw_buffer_store_b64(v2u{pk(ot[0], ot[1]), pk(ot[2], ot[3])}, rWS, (g >> 1) == hp ? lane * 8 + 2048 : OOB, so, SC1);
    DRAIN();
    asm volatile("" ::"v"(tdum));
    if (lane == 0) set_flag(ws, WS_F1 + ((ti * 4 + jt) * 8 + c) * 128 + hp * 4, tag);
    LFLAG_SET(w);
  }
  if (w < 4) {
    LFLAG_WAIT(0, 7);
    const int f1o = WS_F1 + ((ti * 4 + ((lane >> 2) & 3)) * 8 + 2 * jt + ((lane >> 1) & 1)) * 128 + (lane & 1) * 4;
    unsigned pv1 = 0u;
    v4u wf[5][5];
    f32x4 acc2[5];
    {
      const lptr wb = L + L_WO + (160 * jt + 4 * g + (l16 >> 2)) * 160 + (l16 & 3) * 8;
      const lptr wb16 = L + L_WO + (160 * jt + 20 * g + 16 + (l16 >> 2)) * 160 + (l16 & 3) * 8;
#pragma unroll
      for (int ks = 0; ks < 5; ++ks) {
#pragma unroll
        for (int n = 0; n < 5; ++n)
          wf[ks][n] = ks < 4 ? cat(trrd(wb + (ks * 40) * 160 + n * 32), trrd(wb + (ks * 40 + 20) * 160 + n * 32))
                             : cat(trrd(wb16 + n * 32), trrd(wb16 + 80 * 160 + n * 32));
        if (ks == PKS) { SB(); pv1 = __builtin_amdgcn_raw_ptr_buffer_atomic_add_i32(0, rWS, lane < 16 ? f1o : OOB, 0, SC1); SB(); }
      }
#pragma unroll
      for (int n = 0; n < 5; ++n) {
        const v2u xr = *(LAS v2u*)(L + L_XR + sloc * 160 + n * 32 + g * 8);
        acc2[n] = f32x4{blo(xr.x), bhi(xr.x), blo(xr.y), bhi(xr.y)};
      }
      LGKM0();
#pragma unroll
      for (int ks = 0; ks < 5; ++ks) asm volatile("" : "+v"(wf[ks][0]), "+v"(wf[ks][1]), "+v"(wf[ks][2]), "+v"(wf[ks][3]), "+v"(wf[ks][4]), "+v"(acc2[ks]));
      LFLAG_SET(8 + w);
    }
    if (!__all(lane >= 16 || pv1 == tag)) wait_flags<0, true>(ws, f1o, lane < 16, tag, zero);
    __builtin_amdgcn_fence(__ATOMIC_ACQUIRE, "wavefront");
    v4u of[4][4];
    v2u oh[4][2];
#define LD_O(r) _Pragma("unroll") for (int pr = 0; pr < 2; ++pr) { \
      const int so = WS_O + (ti * 8 + 2 * jt + pr) * 10240 + ((jt + 1 + (r)) & 3) * 2560; \
      of[r][2 * pr] = ld128c(rWS, lane * 16, so); of[r][2 * pr + 1] = ld128c(rWS, lane * 16 + 1024, so); \
      oh[r][pr] = __builtin_amdgcn_raw_buffer_load_b64(rWS, lane * 8 + 2048, so, SC1); }
#define LD_O1(r, q) do { const int so = WS_O + (ti * 8 + 2 * jt + ((q) / 3)) * 10240 + ((jt + 1 + (r)) & 3) * 2560; \
      if ((q) % 3 < 2) of[r][2 * ((q) / 3) + (q) % 3] = ld128c(rWS, lane * 16 + ((q) % 3) * 1024, so); \
      else oh[r][(q) / 3] = __builtin_amdgcn_raw_buffer_load_b64(rWS, lane * 8 + 2048, so, SC1); } while (0)
    LD_O(0); SB(); LD_O(1); SB();
    LFLAG_WAIT(8, 3);
    {
      f32x4 t[2][5];
#pragma unroll
      for (int r = 0; r < 4; ++r) {
#pragma unroll
        for (int n = 0; n < 5; ++n) t[r & 1][n] = r < 3 ? f32x4{0.f, 0.f, 0.f, 0.f} : acc2[n];
#pragma unroll
        for (int ks = 0; ks < 5; ++ks) {
          const v4u bq = ks < 4 ? of[r][ks] : cat(oh[r][0], oh[r][1]);
#pragma unroll
          for (int n = 0; n < 5; ++n) {
            t[r & 1][n] = mfma(wf[ks][n], bq, t[r & 1][n]);
            const int i = ks * 5 + n;
            if (r < 2 && i % 4 == 1 && i / 4 < 6) LD_O1(r + 2, i / 4);
            if (r > 0 && i >= 3 && i < 13 && (i & 1))
              *(LAS f32x4*)(L + L_RED2 + ((((jt + r) & 3) * 3 + r - 1) * 5 + (i - 3) / 2) * 1024 + lane * 16) = t[(r - 1) & 1][(i - 3) / 2];
            if (r == 3 && i == 13) { asm volatile("" ::: "memory"); LFLAG_SET(16 + w); }
            SB();
          }
        }
      }
#pragma unroll
      for (int n = 0; n < 5; ++n) acc2[n] = t[1][n];
    }
    LFLAG_WAIT(16, 3);
    {
      const lptr p = L + L_RED2 + (jt * 15) * 1024 + lane * 16;
      f32x4 t[15];
#pragma unroll
      for (int k = 0; k < 15; ++k) t[k] = *(LAS f32x4*)(p + k * 1024);
      SB();
#pragma unroll
      for (int n = 0; n < 5; ++n) acc2[n] = (acc2[n] + t[n]) + (t[5 + n] + t[10 + n]);
      LGKM0();
      LFLAG_SET(20 + w);
    }
    SB();
    float hv[5][4];
    unsigned hp[5][2];
    float s1 = 0.f;
#pragma unroll
    for (int n = 0; n < 5; ++n) {
      const f32x4 v = acc2[n];
      hp[n][0] = pk(v[0], v[1]);
      hp[n][1] = pk(v[2], v[3]);
      hv[n][0] = blo(hp[n][0]); hv[n][1] = bhi(hp[n][0]); hv[n][2] = blo(hp[n][1]); hv[n][3] = bhi(hp[n][1]);
      s1 += (hv[n][0] + hv[n][1]) + (hv[n][2] + hv[n][3]);
    }
    const float mp = sumg(s1) * (1.f / 80.f);
    float m2 = 0.f;
#pragma unroll
    for (int n = 0; n < 5; ++n)
#pragma unroll
      for (int r = 0; r < 4; ++r) { const float d = hv[n][r] - mp; m2 = fmaf(d, d, m2); }
    m2 = sumg(m2);
    const int sto = WS_ST + (ti * 4 + jt) * 2048;
    st128c(v4u{asu(mp), tag, asu(m2), tag}, rWS, g == 0 ? (c * 16 + l16) * 16 : OOB, sto);
#pragma unroll
    for (int n = 0; n < 3; ++n) { hpk[n][0] = hp[n][0]; hpk[n][1] = hp[n][1]; asm volatile("" : "+v"(hpk[n][0]), "+v"(hpk[n][1])); }
    *(LAS v2u*)(L + L_HB + jt * 1024 + lane * 8) = v2u{hp[3][0], hp[3][1]};
    *(LAS v2u*)(L + L_HB + jt * 1024 + 512 + lane * 8) = v2u{hp[4][0], hp[4][1]};
    v4u sv[2];
    __builtin_amdgcn_s_sleep(DS);
#pragma nounroll
    for (int sp = 0; sp < SPIN_CAP; ++sp) {
      sv[0] = ld128c(rWS, lane * 16, sto);
      sv[1] = ld128c(rWS, lane * 16 + 1024, sto);
      const bool ok = (sv[0].y == tag) & (sv[0].w == tag) & (sv[1].y == tag) & (sv[1].w == tag);
      if (__all(ok)) break;
      __builtin_amdgcn_s_sleep(2);
      asm volatile("" ::: "memory");
    }
    const float mean = sumg(asf(sv[0].x) + asf(sv[1].x)) * 0.125f;
    const float d0 = asf(sv[0].x) - mean, d1 = asf(sv[1].x) - mean;
    const float var = sumg(fmaf(fmaf(d0, d0, d1 * d1), 80.f, asf(sv[0].z) + asf(sv[1].z))) * (1.f / 640.f);
    const float rr = __builtin_amdgcn_rsqf(fmaf(1e-5f, var + 1e-5f, var)), nb = -mean * rr;
    const lptr hb = L + L_HN + jt * 2560 + (jt >> 1) * 1280;
#pragma unroll
    for (int n = 0; n < 5; ++n)
      *(LAS v2u*)(hb + l16 * 160 + n * 32 + g * 8) =
          v2u{pk(fmaf(hv[n][0], rr, nb), fmaf(hv[n][1], rr, nb)), pk(fmaf(hv[n][2], rr, nb), fmaf(hv[n][3], rr, nb))};
    asm volatile("" ::: "memory");
#pragma unroll
    for (int a = 0; a < 3; ++a) {
      const int n = (a == 2) ? 4 : 2 * a + (g >> 1);
      const lptr p = hb + (8 * (g & 1) + (l16 >> 2)) * 160 + n * 32 + (l16 & 3) * 8;
      const v2u t0 = trrd(p), t1 = trrd(p + 4 * 160);
      const int vo = (g >> 1) * 65536 + (lane & 31) * 16;
      st128c(cat(t0, t1), rWS, (a == 2 && lane >= 32) ? OOB : vo,
             WS_HN + ((c * 5 + 2 * a) * 64 + 2 * ti + (jt >> 1)) * 1024 + (jt & 1) * 512);
    }
    DRAIN();
    if (lane < 4) set_flag(ws, WS_F2 + lane * 32768 + (c * 32 + ti) * 128 + jt * 4, tag);
    LFLAG_WAIT(12 + jt, 0);
#pragma unroll
    for (int k = 0; k < 3; ++k) { TR_K(w1f[k], L_W1A + jt * 12288 + k * 4096); }
    TR_K(w1f[6], L_STG + jt * 8192); TR_K(w1f[7], L_STG + jt * 8192 + 4096);
    BAR();
#pragma unroll
    for (int k = 3; k < 6; ++k) { TR_K(w1f[k], L_W1B + jt * 12288 + (k - 3) * 4096); }
    TR_K(w2f[0], L_W2A + jt * 4096);
    asm volatile("" ::: "memory");
    LFLAG_SET(24 + w);
  } else {
    const int stg = L_STG + jt * 8192;
    LFLAG_WAIT(0, 7);
    DMA_W1(w, 0, stg, 0);
    DMA_W1(w, 1, stg, 4096);
#pragma unroll
    for (int k = 0; k < 10; ++k) {
      waitv<4>();
      if (k < 8) { TR_K(w1f[k], stg + (k & 1) * 4096); LGKM0(); PIN4(w1f[k]); }
      else { TR_K(w2f[k - 8], stg + (k & 1) * 4096); LGKM0(); PIN4(w2f[k - 8]); }
      if (k < 6) { DMA_W1(w, k + 2, stg, (k & 1) * 4096); }
      else if (k < 8) { DMA_W2(w, k - 6, stg, (k & 1) * 4096); }
      else { DMA_W1(jt, k - 2, stg, (k & 1) * 4096); }
    }
    LFLAG_WAIT(8, 3);
#pragma unroll
    for (int k = 0; k < 3; ++k) { DMA_W1(jt, k, jt * 12288, L_W1A + k * 4096); }
    DRAIN();
    LFLAG_SET(12 + jt);
    LFLAG_WAIT(20, 3);
#pragma unroll
    for (int k = 3; k < 6; ++k) { DMA_W1(jt, k, jt * 12288, L_W1B + (k - 3) * 4096); }
    DMA_W2(jt, 0, jt * 4096, L_W2A);
    DRAIN();
    BAR();
    DMA_W2(w, 2, stg, 0); DMA_W2(w, 3, stg, 4096);
    DMA_W2(w, 4, jt * 12288, L_W1A); DMA_W2(w, 5, jt * 12288, L_W1A + 4096); DMA_W2(jt, 1, jt * 12288, L_W1A + 8192);
    const v2u h3 = *(LAS v2u*)(L + L_HB + jt * 1024 + lane * 8), h4 = *(LAS v2u*)(L + L_HB + jt * 1024 + 512 + lane * 8);
    hpk[0][0] = h3.x; hpk[0][1] = h3.y; hpk[1][0] = h4.x; hpk[1][1] = h4.y; hpk[2][0] = 0u; hpk[2][1] = 0u;
  }
#define LCNT_SET(i, v) do { asm volatile("" ::: "memory"); if (lane == 0) *(volatile LAS unsigned*)(L + L_FLAG + (i) * 4) = (v); } while (0)
#define LCNT_WAIT(i0, mask, v) do { _Pragma("nounroll") for (int sp_ = 0; sp_ < (SPIN_CAP << 4); ++sp_) { \
      if (__all(*(volatile LAS unsigned*)(L + L_FLAG + ((i0) + (lane & (mask))) * 4) >= (v))) break; \
      __builtin_amdgcn_s_sleep(1); } asm volatile("" ::: "memory"); } while (0)
  if (p3on) {
    const int c0 = (dt * 64) / 80, c1 = (dt * 64 + 63) / 80;
    wait_flags<D2, false>(ws, WS_F2 + xi * 32768 + (((lane & 16) ? c1 : c0) * 32 + w * 4 + ((lane >> 2) & 3)) * 128 + (lane & 3) * 4, lane < 32, tag, zero);
    constexpr int RING = 6;
    v4u hf[RING];
#define LD_HN(i) hf[(i) % RING] = ld128c(rWS, lane * 16, WS_HN + ((dt * 4 + ((jt + 1 + ((i) >> 3)) & 3)) * 64 + w * 8 + K8((i) & 7)) * 1024)
#pragma unroll
    for (int i = 0; i < RING - 1; ++i) LD_HN(i);
    SB();
    LFLAG_WAIT(24, 3);
    f32x4 a3[4];
    v2u b1v[4];
#pragma unroll
    for (int sg = 0; sg < 4; ++sg) {
#pragma unroll
      for (int j = 0; j < 4; ++j) a3[j] = f32x4{0.f, 0.f, 0.f, 0.f};
#pragma unroll
      for (int k = 0; k < 8; ++k) {
        const int i = sg * 8 + k;
        if (i + RING - 1 < 32) LD_HN(i + RING - 1);
        if (i == 8) {
#pragma unroll
          for (int j = 0; j < 4; ++j) b1v[j] = *(const GAS v2u*)(b1 + 64 * ft + 16 * j + 4 * g);
        }
#pragma unroll
        for (int j = 0; j < 4; ++j) a3[j] = mfma(w1f[k][j], hf[i % RING], a3[j]);
        SB();
      }
      const lptr p = L + L_PA + (((jt + 1 + sg) & 3) * 16 + jt * 4) * 1024 + lane * 16;
      if (w < 4) {
        if (sg == 3) {
          LCNT_WAIT(16, 3, 2u);
          f32x4 t[12];
#pragma unroll
          for (int k = 0; k < 12; ++k) t[k] = *(LAS f32x4*)(L + L_PA + (jt * 16 + ((jt + 1 + (k >> 2)) & 3) * 4 + (k & 3)) * 1024 + lane * 16);
#pragma unroll
          for (int j = 0; j < 4; ++j) a3[j] += (t[j] + t[4 + j]) + (t[8 + j] + f32x4{blo(b1v[j].x), bhi(b1v[j].x), blo(b1v[j].y), bhi(b1v[j].y)});
          asm volatile("" : "+v"(a3[0]), "+v"(a3[1]), "+v"(a3[2]), "+v"(a3[3]));
        }
        LCNT_WAIT(28 + jt, 0, sg + 1u);
#pragma unroll
        for (int j = 0; j < 4; ++j) a3[j] += *(LAS f32x4*)(p + j * 1024);
      }
      if (sg < 3 || w >= 4) {
#pragma unroll
        for (int j = 0; j < 4; ++j) *(LAS f32x4*)(p + j * 1024) = a3[j];
      }
      if (w >= 4) LCNT_SET(28 + jt, sg + 1u);
      else if (sg == 2) LCNT_SET(16 + jt, 2u);
    }
    if (w < 4) {
#pragma unroll
      for (int a = 0; a < 2; ++a) {
        v4u uo;
#pragma unroll
        for (int e = 0; e < 4; ++e) {
          const f32x2 v = {a3[2 * a + (e >> 1)][2 * (e & 1)], a3[2 * a + (e >> 1)][2 * (e & 1) + 1]};
          const f32x2 k1 = {-2.302208198f, -2.302208198f}, k3 = {-0.1029432397f, -0.1029432397f};
          const f32x2 zz = v * __builtin_elementwise_fma(v * v, k3, k1);
          const f32x2 dn = f32x2{__builtin_amdgcn_exp2f(zz[0]), __builtin_amdgcn_exp2f(zz[1])} + f32x2{1.f, 1.f};
          const f32x2 gl = v * f32x2{__builtin_amdgcn_rcpf(dn[0]), __builtin_amdgcn_rcpf(dn[1])};
          uo[e] = pk(gl[0], gl[1]);
        }
        st128c(uo, rWS, lane * 16, WS_U + ((dt * 4 + jt) * 48 + 2 * ft + a) * 1024);
      }
      DRAIN();
      if (lane < 4) set_flag(ws, WS_F3 + lane * 32768 + (dt * 24 + ft) * 128 + jt * 4, tag);
    }
  }
  {
    const int stg = L_STG + jt * 8192, sa = L_W1A + jt * 12288;
    if (w >= 4) {
      DRAIN();
      TR_K(w2f[2], stg); TR_K(w2f[3], stg + 4096); TR_K(w2f[4], sa); TR_K(w2f[5], sa + 4096);
      LGKM0();
      PIN4(w2f[2]); PIN4(w2f[3]); PIN4(w2f[4]); PIN4(w2f[5]);
      DMA_W2(jt, 2, stg, 0); DMA_W2(jt, 3, stg, 4096); DMA_W2(jt, 4, sa, 0); DMA_W2(jt, 5, sa, 4096);
      DRAIN();
      BAR();
    } else {
      BAR();
      TR_K(w2f[1], sa + 8192); TR_K(w2f[2], stg); TR_K(w2f[3], stg + 4096); TR_K(w2f[4], sa); TR_K(w2f[5], sa + 4096);
    }
    const float b2v = blo(*(const GAS u16*)(b2 + stok));
    {
      const int n = lane / 3, k = lane - n * 3, d16 = c * 5 + n;
      wait_flags<0, true>(ws, WS_F3 + xi * 32768 + ((d16 >> 2) * 24 + 3 * w + k) * 128 + (d16 & 3) * 4, lane < 15, tag, zero);
    }
    constexpr int RING = 6;
    v4u uf[RING];
#define LD_U(i) uf[(i) % RING] = ld128c(rWS, lane * 16, WS_U + ((c * 5 + (i) / 6) * 48 + w * 6 + K6((i) % 6)) * 1024)
#pragma unroll
    for (int i = 0; i < RING - 1; ++i) LD_U(i);
    SB();
    BAR();
#pragma unroll
    for (int n = 0; n < 5; ++n) {
      f32x4 a4[4];
#pragma unroll
      for (int j = 0; j < 4; ++j) a4[j] = f32x4{0.f, 0.f, 0.f, 0.f};
#pragma unroll
      for (int k = 0; k < 6; ++k) {
        const int i = n * 6 + k;
        if (i + RING - 1 < 30) LD_U(i + RING - 1);
#pragma unroll
        for (int j = 0; j < 4; ++j) a4[j] = mfma(uf[i % RING], w2f[k][j], a4[j]);
        SB();
      }
#pragma unroll
      for (int j = 0; j < 4; ++j) *(LAS f32x4*)(L + ((n * 4 + j) * 8 + w) * 1024 + lane * 16) = a4[j];
    }
    BAR();
    const int no = w < 4 ? 0 : 3;
#pragma unroll
    for (int nn = 0; nn < 3; ++nn) {
      if (nn == 2 && w >= 4) break;
      const int n = nn + no;
      const lptr p = L + ((n * 4 + (sloc >> 4)) * 8) * 1024 + (g * 16 + (sloc & 15)) * 16;
      f32x4 t[8];
#pragma unroll
      for (int k = 0; k < 8; ++k) t[k] = *(LAS f32x4*)(p + k * 1024);
      const f32x4 v = ((t[0] + t[1]) + (t[2] + t[3])) + ((t[4] + t[5]) + (t[6] + t[7]));
      const v2u o = {pk(v[0] + b2v + blo(hpk[nn][0]), v[1] + b2v + bhi(hpk[nn][0])), pk(v[2] + b2v + blo(hpk[nn][1]), v[3] + b2v + bhi(hpk[nn][1]))};
      __builtin_amdgcn_raw_buffer_store_b64(o, __builtin_amdgcn_make_buffer_rsrc((void*)out, 0, 2048 * 640 * 2, 0x00020000), stok * 1280 + c * 160 + n * 32 + g * 8, 0, 16);
    }
  }
  if (b == 0 && tid == 0) set_flag(ws, WS_EPOCH, tag);
  asm volatile(".Lk_end:");
}

extern "C" void kernel_launch(void* const* d_in, const int* in_sizes, int n_in, void* d_out, int out_size, void* d_ws,
                              size_t ws_size, hipStream_t stream) {
  (void)hipFuncSetAttribute((const void*)fused_kernel, hipFuncAttributeMaxDynamicSharedMemorySize, LDS_BYTES);
  fused_kernel<<<256, 512, LDS_BYTES, stream>>>((const u16*)d_in[0], (const u16*)d_in[1], (const u16*)d_in[2], (const u16*)d_in[4],
                                                (const u16*)d_in[5], (const u16*)d_in[6], (const u16*)d_in[7], (u16*)d_out, (char*)d_ws);
}
```
